```python
import torch

def _softmax(x, axis=-1):
    return torch.softmax(x, dim=axis)
def _gelu(x):
    return torch.nn.functional.gelu(x, approximate="tanh")
def _silu(x):
    return torch.nn.functional.silu(x)
def _sigmoid(x):
    return torch.sigmoid(x)
def _layernorm(x, axis=-1, eps=1e-5):
    m = x.mean(dim=axis, keepdim=True)
    v = x.var(dim=axis, keepdim=True, unbiased=False)
    return (x - m) / torch.sqrt(v + eps)
def _scatter_add(tgt, idx, src, axis=0):
    return tgt.clone().index_add_(axis, idx, src)
def _segment_sum(src, seg, n):
    out = torch.zeros((n,) + src.shape[1:], dtype=src.dtype)
    return out.index_add_(0, seg, src)
def _pad(x, pw):
    flat = tuple(v for pair in reversed(pw) for v in pair)
    return torch.nn.functional.pad(x, flat)

kernel_name = 'concat_heads__gqa_block_cached__sliding_block_cached__decode'

def setup_inputs():

    _w = (lambda a: (lambda t: t.to(torch.bfloat16) if t.is_floating_point() else t)(torch.as_tensor(a)))
    def _A_setup():
        B, Sq, D = 1, 1, 1024
        Hq, Hkv, Sk, Dh = 32, 4, 65536, 128
        assert Hq % Hkv == 0, (Hq, Hkv)
        return {
            "x": _w((lambda *s: torch.randn(*s, dtype=torch.bfloat16) * 0.4)(B, Sq, D)),
            "k_cache": _w((lambda *s: torch.randn(*s, dtype=torch.bfloat16) * 3.2)(B, Hkv, Sk, Dh)),
            "v_cache": _w((lambda *s: torch.randn(*s, dtype=torch.bfloat16) * 0.8)(B, Hkv, Sk, Dh)),
            "wq": _w((lambda *s: torch.randn(*s, dtype=torch.bfloat16) * 3.2)(D, Hq * Dh) * (D ** -0.5)),
            "wo": _w((lambda *s: torch.randn(*s, dtype=torch.bfloat16) * 0.8)(Hq * Dh, D) * ((Hq * Dh) ** -0.5)),
        }
    _A_in = _A_setup()

    def _B_setup():
        B, Sq, D = 1, 1, 1024
        Hq, Hkv, Sk, Dh = 32, 4, 65536, 128
        assert Hq % Hkv == 0, (Hq, Hkv)
        return {
            "x": _w((lambda *s: torch.randn(*s, dtype=torch.bfloat16) * 0.2)(B, Sq, D)),
            "k_cache": _w((lambda *s: torch.randn(*s, dtype=torch.bfloat16) * 3.2)(B, Hkv, Sk, Dh)),
            "v_cache": _w((lambda *s: torch.randn(*s, dtype=torch.bfloat16) * 1.6)(B, Hkv, Sk, Dh)),
            "wq": _w((lambda *s: torch.randn(*s, dtype=torch.bfloat16) * 3.2)(D, Hq * Dh) * (D ** -0.5)),
            "wo": _w((lambda *s: torch.randn(*s, dtype=torch.bfloat16) * 1.6)(Hq * Dh, D) * ((Hq * Dh) ** -0.5)),
        }
    _B_in = _B_setup()

    return {
        "A_x": _A_in["x"],
        "A_k_cache": _A_in["k_cache"],
        "A_v_cache": _A_in["v_cache"],
        "A_wq": _A_in["wq"],
        "A_wo": _A_in["wo"],
        "B_x": _B_in["x"],
        "B_k_cache": _B_in["k_cache"],
        "B_v_cache": _B_in["v_cache"],
        "B_wq": _B_in["wq"],
        "B_wo": _B_in["wo"],
    }

def reference(A_x, A_k_cache, A_v_cache, A_wq, A_wo, B_x, B_k_cache, B_v_cache, B_wq, B_wo):
    def _A_ref(x, k_cache, v_cache, wq, wo):
        B, Sq, D = x.shape
        Hq, Dh = 32, 128
        Hkv = k_cache.shape[1]
        rep = Hq // Hkv
        q = (lambda x, *d: x.permute(*d))((x @ wq).reshape(B, Sq, Hq, Dh), 0, 2, 1, 3)
        k = (lambda x, n, axis: torch.repeat_interleave(x, n, dim=axis))(k_cache, rep, axis=1)
        v = (lambda x, n, axis: torch.repeat_interleave(x, n, dim=axis))(v_cache, rep, axis=1)
        scores = torch.einsum("bhqd,bhkd->bhqk", q, k) * (Dh ** -0.5)
        p = _softmax(scores, axis=-1)
        o = torch.einsum("bhqk,bhkd->bhqd", p, v)
        return (lambda x, *d: x.permute(*d))(o, 0, 2, 1, 3).reshape(B, Sq, Hq * Dh) @ wo

    def _B_ref(x, k_cache, v_cache, wq, wo):
        B, Sq, D = x.shape
        Hq, Dh = 32, 128
        Hkv, Sk = k_cache.shape[1], k_cache.shape[2]
        W = min(4096, max(256, Sk // 4))
        rep = Hq // Hkv
        q = (lambda x, *d: x.permute(*d))((x @ wq).reshape(B, Sq, Hq, Dh), 0, 2, 1, 3)
        k = (lambda x, n, axis: torch.repeat_interleave(x, n, dim=axis))(k_cache[:, :, Sk - W:, :], rep, axis=1)
        v = (lambda x, n, axis: torch.repeat_interleave(x, n, dim=axis))(v_cache[:, :, Sk - W:, :], rep, axis=1)
        scores = torch.einsum("bhqd,bhkd->bhqk", q, k) * (Dh ** -0.5)
        p = _softmax(scores, axis=-1)
        o = torch.einsum("bhqk,bhkd->bhqd", p, v)
        return (lambda x, *d: x.permute(*d))(o, 0, 2, 1, 3).reshape(B, Sq, Hq * Dh) @ wo

    a = _A_ref(A_x, A_k_cache, A_v_cache, A_wq, A_wo)
    b = _B_ref(B_x, B_k_cache, B_v_cache, B_wq, B_wo)
    return torch.concatenate([a, b], axis=-1)
```

```cpp
#include <hip/hip_runtime.h>
#include <hip/hip_bf16.h>
#include <stdint.h>

typedef unsigned short u16;
typedef unsigned v4u __attribute__((ext_vector_type(4)));
__device__ __forceinline__ float bf2f(u16 b) { return __uint_as_float(((unsigned)b) << 16); }
__device__ __forceinline__ float lo(unsigned u) { return __uint_as_float(u << 16); }
__device__ __forceinline__ float hi(unsigned u) { return __uint_as_float(u & 0xffff0000u); }
__device__ __forceinline__ float rbf(float f) { return __bfloat162float(__float2bfloat16(f)); }

constexpr int D = 1024, HQ = 32, HKV = 4, SK = 65536, DH = 128, W = 4096;
constexpr int NSA = 64;
constexpr int NSB = 4;

__global__ void qproj(const u16* xa, const u16* wqa, const u16* xb, const u16* wqb, float* q) {
  int p = blockIdx.y; const u16* x = p ? xb : xa; const u16* wq = p ? wqb : wqa;
  int col = blockIdx.x * 64 + (threadIdx.x & 63); int part = threadIdx.x >> 6;
  float acc = 0.f;
  for (int r = part * 256; r < part * 256 + 256; ++r) acc += bf2f(x[r]) * bf2f(wq[(size_t)r * 4096 + col]);
  __shared__ float s[256]; s[threadIdx.x] = acc; __syncthreads();
  if (part == 0) q[p * 4096 + col] = rbf(s[threadIdx.x] + s[threadIdx.x + 64] + s[threadIdx.x + 128] + s[threadIdx.x + 192]);
}

__global__ void attn(const u16* ka, const u16* va, const u16* kb, const u16* vb, const float* q, float* pm, float* pl, float* po) {
  int b = blockIdx.x; int p, g, s; const u16 *k, *v;
  if (b < HKV * NSA) { p = 0; g = b / NSA; s = b % NSA; k = ka + ((size_t)g * SK + s * 1024) * DH; v = va + ((size_t)g * SK + s * 1024) * DH; }
  else { b -= HKV * NSA; p = 1; g = b / NSB; s = b % NSB; k = kb + ((size_t)g * SK + SK - W + s * 1024) * DH; v = vb + ((size_t)g * SK + SK - W + s * 1024) * DH; }
  __shared__ float sq[8][128]; __shared__ float sc[8][1024]; __shared__ float red[8][4];
  for (int i = threadIdx.x; i < 1024; i += 256) sq[i >> 7][i & 127] = q[p * 4096 + g * 1024 + i];
  __syncthreads();
  for (int key = threadIdx.x; key < 1024; key += 256) {
    float a[8] = {0, 0, 0, 0, 0, 0, 0, 0};
    const v4u* kr = (const v4u*)(k + (size_t)key * DH);
    for (int c = 0; c < 16; ++c) { v4u w = kr[c];
      for (int j = 0; j < 4; ++j) { float l = lo(w[j]), h = hi(w[j]);
        for (int hh = 0; hh < 8; ++hh) a[hh] += l * sq[hh][c * 8 + 2 * j] + h * sq[hh][c * 8 + 2 * j + 1]; } }
    for (int hh = 0; hh < 8; ++hh) sc[hh][key] = a[hh] * 0.08838834764831845f;
  }
  __syncthreads();
  int wv = threadIdx.x >> 6, ln = threadIdx.x & 63;
  for (int hh = wv; hh < 8; hh += 4) {
    float m = -INFINITY; for (int i = ln; i < 1024; i += 64) m = fmaxf(m, sc[hh][i]);
    for (int o = 32; o; o >>= 1) m = fmaxf(m, __shfl_xor(m, o));
    float l = 0; for (int i = ln; i < 1024; i += 64) { float e = __expf(sc[hh][i] - m); sc[hh][i] = e; l += e; }
    for (int o = 32; o; o >>= 1) l += __shfl_xor(l, o);
    if (ln == 0) { int idx = (p * 32 + g * 8 + hh) * 64 + s; pm[idx] = m; pl[idx] = l; }
  }
  __syncthreads();
  { int d = threadIdx.x & 127, half = threadIdx.x >> 7; float a[4] = {0, 0, 0, 0};
    for (int key = 0; key < 1024; ++key) { float vv = bf2f(v[(size_t)key * DH + d]);
      for (int j = 0; j < 4; ++j) a[j] += sc[half * 4 + j][key] * vv; }
    for (int j = 0; j < 4; ++j) po[((size_t)(p * 32 + g * 8 + half * 4 + j) * 64 + s) * 128 + d] = a[j]; }
}

__global__ void combine(const float* pm, const float* pl, const float* po, float* o) {
  int ph = blockIdx.x; int p = ph >> 5; int ns = p ? NSB : NSA; int d = threadIdx.x;
  float M = -INFINITY; for (int s = 0; s < ns; ++s) M = fmaxf(M, pm[ph * 64 + s]);
  float L = 0, a = 0; for (int s = 0; s < ns; ++s) { float w = __expf(pm[ph * 64 + s] - M); L += w * pl[ph * 64 + s]; a += w * po[((size_t)ph * 64 + s) * 128 + d]; }
  o[ph * 128 + d] = rbf(a / L);
}

__global__ void oproj(const float* o, const u16* woa, const u16* wob, __hip_bfloat16* out) {
  int p = blockIdx.y; const u16* wo = p ? wob : woa;
  int col = blockIdx.x * 64 + (threadIdx.x & 63); int part = threadIdx.x >> 6;
  float acc = 0.f;
  for (int r = part * 256; r < part * 256 + 256; ++r) acc += o[p * 4096 + r] * bf2f(wo[(size_t)r * 1024 + col]);
  __shared__ float s[1024]; s[threadIdx.x] = acc; __syncthreads();
  if (part == 0) { float t = 0; for (int i = 0; i < 16; ++i) t += s[threadIdx.x + 64 * i]; out[p * 1024 + col] = __float2bfloat16(t); }
}

extern "C" void kernel_launch(void* const* d_in, const int* in_sizes, int n_in, void* d_out, int out_size, void* d_ws, size_t ws_size, hipStream_t stream) {
  const u16 *xa = (const u16*)d_in[0], *ka = (const u16*)d_in[1], *va = (const u16*)d_in[2], *wqa = (const u16*)d_in[3], *woa = (const u16*)d_in[4];
  const u16 *xb = (const u16*)d_in[5], *kb = (const u16*)d_in[6], *vb = (const u16*)d_in[7], *wqb = (const u16*)d_in[8], *wob = (const u16*)d_in[9];
  float* q = (float*)d_ws; float* pm = q + 8192; float* pl = pm + 4096; float* o = pl + 4096; float* po = o + 8192;
  qproj<<<dim3(64, 2), 256, 0, stream>>>(xa, wqa, xb, wqb, q);
  attn<<<HKV * NSA + HKV * NSB, 256, 0, stream>>>(ka, va, kb, vb, q, pm, pl, po);
  combine<<<64, 128, 0, stream>>>(pm, pl, po, o);
  oproj<<<dim3(16, 2), 1024, 0, stream>>>(o, woa, wob, (__hip_bfloat16*)d_out);
}
```

```cpp
#include <hip/hip_runtime.h>
#include <hip/hip_bf16.h>
#include <stdint.h>

#define GAS __attribute__((address_space(1)))
#define LAS __attribute__((address_space(3)))
typedef unsigned short u16;
typedef unsigned long long u64;
typedef unsigned v4u __attribute__((ext_vector_type(4)));
typedef unsigned v2u __attribute__((ext_vector_type(2)));
typedef float v4f __attribute__((ext_vector_type(4)));
typedef float v2f __attribute__((ext_vector_type(2)));
typedef short v4s __attribute__((ext_vector_type(4)));
typedef __bf16 v8bf __attribute__((ext_vector_type(8)));
typedef __bf16 v2bf __attribute__((ext_vector_type(2)));

constexpr int INFL = 3;
constexpr int QTRIG = 17;
constexpr int OTRIG = 6;
constexpr int NSTAT = 28;
constexpr int CPF = 24;
constexpr int NWG = 256, NTHR = 320;
constexpr int SK = 65536, WIN = 4096;
constexpr int NSLOT = 16, SLOT = 8192;
constexpr int U_WQ = 8, U_WO = 8;
constexpr int U_TILE0 = U_WQ;
constexpr int TM = 8;
constexpr int RWIN = 6;
constexpr int NTB = 16;
static_assert(INFL >= 3 && INFL <= 4 && NSTAT % 4 == 0 && NSTAT - 4 >= NTB && NSTAT <= 28, "");
constexpr int POOL = 16 * 34 - 16 * NSTAT;
constexpr int L_FLAG = NSLOT * SLOT;
constexpr int F_LANDED = 0, F_NT = 1, F_QA = 2, F_QB = 3, F_BM = 4, F_O = 5, F_PUB = 6, F_ST = 8, F_ACK = 12, F_FREE = 16, F_TAG = 32;
constexpr int L_S = L_FLAG + 256;
constexpr int L_X = L_S;
constexpr int L_Q = L_S + 4 * 7 * 512;
constexpr int QROW = 272, QSZ = 8 * QROW;
constexpr int STSZ = 4096 + 64;
constexpr int L_ST = L_Q + 2 * QSZ;
constexpr int L_O = L_ST + 3 * STSZ;
constexpr int LDS_BYTES = L_O + 64;
constexpr unsigned W_EPOCH = 0;
constexpr unsigned W_CNT = 256;
constexpr unsigned W_QG = 8192;
constexpr unsigned W_LSB = W_QG + 2 * 4 * 2 * 8192;
constexpr unsigned W_POB = W_LSB + 4 * 8 * 128;
constexpr unsigned W_X = W_POB + 4 * 32 * 8 * 128, XB = 66 * 128, X_PO = 4096, X_LS = 8192;
constexpr unsigned W_END = W_X + 256 * XB;
static inline size_t ws_base(size_t ws_size) { return (ws_size - W_END) & ~(size_t)4095; }
constexpr float SCALE = 0.08838834764831845f, LOG2E = 1.4426950408889634f;
constexpr int SPIN_LDS = 1 << 18, SPIN_GLB = 1 << 13;

struct Params {
  const u16 *xa, *ka, *va, *wqa, *woa, *xb, *kb, *vb, *wqb, *wob;
  u16* out; unsigned char* ws;
};

__device__ __forceinline__ v4f mfma(v4u a, v4u b, v4f c) {
  return __builtin_amdgcn_mfma_f32_16x16x32_bf16(__builtin_bit_cast(v8bf, a), __builtin_bit_cast(v8bf, b), c, 0, 0, 0);
}
__device__ __forceinline__ unsigned pk_bf16(float lo, float hi) { v2f x = {lo, hi}; return __builtin_bit_cast(unsigned, __builtin_convertvector(x, v2bf)); }
__device__ __forceinline__ float lo_f(unsigned u) { return __builtin_bit_cast(float, u << 16); }
__device__ __forceinline__ float hi_f(unsigned u) { return __builtin_bit_cast(float, u & 0xffff0000u); }
__device__ __forceinline__ float f_of(unsigned u) { return __builtin_bit_cast(float, u); }
__device__ __forceinline__ unsigned u_of(float f) { return __builtin_bit_cast(unsigned, f); }
__device__ __forceinline__ float vmax(float a, float b) { return __builtin_elementwise_maximum(a, b); }
template <int CTRL> __device__ __forceinline__ float dpp(float x) {
  return __builtin_bit_cast(float, __builtin_amdgcn_mov_dpp(__builtin_bit_cast(int, x), CTRL, 0xf, 0xf, true));
}
constexpr int ROR1 = 0x121, ROR2 = 0x122, ROR4 = 0x124, ROR8 = 0x128, NEWBCAST = 0x150;
__device__ __forceinline__ float x16_max(float x) {
  auto s = __builtin_amdgcn_permlane16_swap(u_of(x), u_of(x), false, false);
  x = vmax(f_of(s[0]), f_of(s[1]));
  auto t = __builtin_amdgcn_permlane32_swap(u_of(x), u_of(x), false, false);
  return vmax(f_of(t[0]), f_of(t[1]));
}
__device__ __forceinline__ float x16_sum(float x) {
  auto s = __builtin_amdgcn_permlane16_swap(u_of(x), u_of(x), false, false);
  x = f_of(s[0]) + f_of(s[1]);
  auto t = __builtin_amdgcn_permlane32_swap(u_of(x), u_of(x), false, false);
  return f_of(t[0]) + f_of(t[1]);
}
__device__ __forceinline__ float x16_sum4(float a, float b, float c, float d) {
  auto s = __builtin_amdgcn_permlane16_swap(u_of(a), u_of(b), false, false);
  auto t = __builtin_amdgcn_permlane16_swap(u_of(c), u_of(d), false, false);
  auto r = __builtin_amdgcn_permlane32_swap(u_of(f_of(s[0]) + f_of(s[1])), u_of(f_of(t[0]) + f_of(t[1])), false, false);
  return f_of(r[0]) + f_of(r[1]);
}
__device__ __forceinline__ float ex2(float x) { return __builtin_amdgcn_exp2f(x); }
__device__ __forceinline__ v4u pack4(v4f x, unsigned ta, unsigned tb) {
  return v4u{__builtin_amdgcn_perm(u_of(x[1]), u_of(x[0]), 0x05030201u), __builtin_amdgcn_perm(ta, u_of(x[1]), 0x07060302u),
             __builtin_amdgcn_perm(u_of(x[3]), u_of(x[2]), 0x05030201u), __builtin_amdgcn_perm(tb, u_of(x[3]), 0x07060302u)};
}
__device__ __forceinline__ v4f unpack4(v4u v) {
  return v4f{f_of(v.x << 8), f_of(__builtin_amdgcn_perm(v.y, v.x, 0x0504030cu)), f_of(v.z << 8), f_of(__builtin_amdgcn_perm(v.w, v.z, 0x0504030cu))};
}

__device__ __forceinline__ unsigned lds_rd(unsigned addr) {
  unsigned r; asm volatile("ds_read_b32 %0, %1\n\ts_waitcnt lgkmcnt(0)" : "=v"(r) : "v"(addr) : "memory");
  return __builtin_amdgcn_readfirstlane(r);
}
__device__ __forceinline__ void lds_wr(unsigned addr, unsigned v) { asm volatile("ds_write_b32 %0, %1" :: "v"(addr), "v"(v) : "memory"); }
__device__ __forceinline__ unsigned lds_wait_ge(unsigned addr, unsigned want) {
  unsigned v;
#pragma unroll 1
  for (int i = 0; i < SPIN_LDS; ++i) { v = lds_rd(addr); if (v >= want) break; __builtin_amdgcn_s_sleep(1); }
  return v;
}
__device__ __forceinline__ v4u lds128(unsigned a) { return *(const LAS v4u*)(size_t)a; }
__device__ __forceinline__ v2u ldstr(unsigned a) { return __builtin_bit_cast(v2u, __builtin_amdgcn_ds_read_tr16_b64_v4i16((LAS v4s*)(size_t)a)); }
#define DMA16(gptr, ldsaddr, imm) __builtin_amdgcn_global_load_lds((const GAS unsigned*)(gptr), (LAS unsigned*)(size_t)(ldsaddr), 16, imm, 2)
#define DMA4(base, a0, a1, a2, a3, ldsaddr) asm volatile("s_mov_b32 m0, %5\n\ts_nop 0\n\tglobal_load_lds_dwordx4 %0, %4 nt\n\tglobal_load_lds_dwordx4 %1, %4 offset:1024 nt\n\t" \
    "global_load_lds_dwordx4 %2, %4 offset:2048 nt\n\tglobal_load_lds_dwordx4 %3, %4 offset:3072 nt" :: "v"(a0), "v"(a1), "v"(a2), "v"(a3), "s"(base), "s"(ldsaddr) : "memory")
#define VMCNT(n) asm volatile("s_waitcnt vmcnt(%0)" :: "i"(n) : "memory")
#define SCHED() __builtin_amdgcn_sched_barrier(0)
#define GLD(dst, voff, imm) asm volatile("buffer_load_dwordx4 %0, %1, %2, 0 offen offset:%3 sc1" : "+v"(dst) : "v"(voff), "s"(ws), "i"(imm) : "memory")
#define GST(val, voff) __builtin_amdgcn_raw_buffer_store_b128(val, ws, voff, 0, 16)

__global__ void __launch_bounds__(NTHR, 2) fused_decode(Params p) {
  u64 pc0; asm volatile("s_getpc_b64 %0" : "=s"(pc0));
  const int lane = threadIdx.x & 63, wave = __builtin_amdgcn_readfirstlane(threadIdx.x >> 6);
  const int b = blockIdx.x;
  const int g = (b >> 3) & 3, j = (b & 7) | ((b >> 5) << 3);
  constexpr unsigned FL = L_FLAG;
  const int P = j >> 5;
  const bool isb = (j >> 5) == (j & 1) && ((j >> 3) & 3) == ((j & 7) >> 1);

  const __amdgpu_buffer_rsrc_t ws = __builtin_amdgcn_make_buffer_rsrc(p.ws, 0, W_END, 0x00020000);
  const unsigned prod = g * 32 + (j & 31);
  const unsigned rot = (b * 2654435761u) >> 29;

  if (__builtin_expect(wave == 0, 1)) {
    lds_wr(FL + lane * 4, 0u);
    int pub = 0;
    auto ladder = [&](int o) {
      asm volatile("s_waitcnt vmcnt(48)\n\ts_cmp_gt_i32 %0, 5\n\ts_cbranch_scc1 1f\n\ts_waitcnt vmcnt(40)\n\ts_cmp_eq_u32 %0, 5\n\ts_cbranch_scc1 1f\n\t"
                   "s_waitcnt vmcnt(32)\n\ts_cmp_eq_u32 %0, 4\n\ts_cbranch_scc1 1f\n\ts_waitcnt vmcnt(24)\n\ts_cmp_eq_u32 %0, 3\n\ts_cbranch_scc1 1f\n\t"
                   "s_waitcnt vmcnt(16)\n\ts_cmp_eq_u32 %0, 2\n\ts_cbranch_scc1 1f\n\ts_waitcnt vmcnt(8)\n\ts_cmp_eq_u32 %0, 1\n\ts_cbranch_scc1 1f\n\t"
                   "s_waitcnt vmcnt(0)\n1:" :: "s"(o) : "scc", "memory");
    };
    auto pre = [&](int u) {
      const unsigned fa = FL + (F_FREE + (u & (NSLOT - 1))) * 4, need = u - NSLOT + 1;
      if (u >= NSLOT && lds_rd(fa) < need) { VMCNT(0); pub = u; lds_wr(FL + F_LANDED * 4, pub); lds_wait_ge(fa, need); }
    };
    auto pace = [&](int u, int win) {
      while (u - pub > win) { ladder(u - pub - 1); ++pub; lds_wr(FL + F_LANDED * 4, pub); }
    };
    {
      const GAS unsigned char* s = (const GAS unsigned char*)(P ? p.wqb : p.wqa) + ((size_t)((j & 1) * 512 + (lane >> 3)) * 4096 + g * 1024 + ((j >> 1) & 15) * 64) * 2;
      const unsigned e0 = ((lane & 7) ^ ((lane >> 3) & 2)) << 4;
      const GAS unsigned char* s0 = s + e0 + rot * (64 * 8192);
      const GAS unsigned char* s1 = s + (e0 ^ 64) + 8 * 8192 + rot * (64 * 8192);
#pragma unroll 1
      for (int u = 0; u < U_WQ; ++u) {
        if (u == 7) { VMCNT(48); pub = 1; lds_wr(FL + F_LANDED * 4, pub); }
#pragma unroll
        for (int i = 0; i < 4; ++i) { DMA16(s0 + i * 16 * 8192, u * SLOT + i * 2048, 0); DMA16(s1 + i * 16 * 8192, u * SLOT + i * 2048 + 1024, 0); }
        s0 += 64 * 8192; s1 += 64 * 8192;
        if (u + rot == 7) { s0 -= 512 * 8192; s1 -= 512 * 8192; }
        if (u == 6) { asm volatile("s_waitcnt lgkmcnt(0)" ::: "memory"); __builtin_amdgcn_s_barrier(); }
      }
    }
    unsigned ko[4], vo[2];
#pragma unroll
    for (int i = 0; i < 4; ++i) { const int row = 4 * i + (lane >> 4); ko[i] = (lane >> 4) * 256 + (((lane & 15) ^ row) << 4); }
#pragma unroll
    for (int i = 0; i < 2; ++i) { const int row = 4 * i + (lane >> 4); vo[i] = (lane >> 4) * 256 + (((lane & 15) ^ (row << 1)) << 4); }
    const GAS unsigned char* kb = (const GAS unsigned char*)p.ka + (size_t)g * SK * 256;
    const GAS unsigned char* vb = (const GAS unsigned char*)p.va + (size_t)g * SK * 256;
    int u = U_TILE0, win = INFL;
    auto unit = [&](const GAS unsigned char* src, bool isv) {
      pre(u);
      if (__builtin_expect(u - pub == INFL + 1 && win == INFL, 1)) { VMCNT(8 * INFL); ++pub; lds_wr(FL + F_LANDED * 4, pub); } else pace(u, win);
      const unsigned d = (u & (NSLOT - 1)) * SLOT;
#pragma unroll
      for (int hf = 0; hf < 2; ++hf) {
        const GAS unsigned char* s = src + hf * 4096;
        const unsigned a0 = isv ? vo[0] : ko[0], a1 = isv ? vo[1] : ko[1], a2 = isv ? vo[0] : ko[2], a3 = isv ? vo[1] : ko[3];
        DMA4(s, a0, a1, a2, a3, d + hf * 4096);
      }
      ++u;
    };
    const GAS unsigned* cnt = (const GAS unsigned*)(p.ws + W_CNT + (g * 4 + ((j >> 3) & 3)) * 256);
    const unsigned one = 1u;
    auto ask = [&](int n) {
      asm volatile("s_mov_b64 exec, %1\n\tglobal_atomic_add v250, %0, %2, %3 sc0 sc1\n\ts_mov_b64 exec, -1" :: "v"(0u), "s"(1ull << (n & 7)), "v"(one), "s"(cnt) : "memory", "v250");
    };
    auto place = [&](unsigned w, unsigned i) {
      const unsigned r = ((g * 64 + w) * 2654435761u) >> 20;
      return ((i / TM) * (64 * TM) + w * TM + (i + r) % TM) * 8192;
    };
    int nt = 1 << 20;
    auto answer = [&](int n) {
      VMCNT(32);
      unsigned t; asm volatile("v_readlane_b32 %0, v250, %1" : "=s"(t) : "s"(n & 7) : "memory");
      if (t >= POOL && n < nt) { nt = n; lds_wr(FL + F_NT * 4, nt); win = RWIN; }
      const unsigned pl = (j >> 3) & 3, k = t - 2 * NTB;
      const unsigned w = t < 2 * NTB ? (2 * pl + (t & 1)) | pl << 3 | (t & 1) << 5 : (k & 7) | pl << 3 | (k & 8) << 2;
      const unsigned i = t < 2 * NTB ? (NSTAT - NTB) + (t >> 1) : NSTAT + (k >> 4);
      return place(w, i);
    };
#pragma unroll 1
    for (int h = isb ? 0 : 2; h < 4; ++h) {
      const GAS unsigned char* src = h < 2 ? (const GAS unsigned char*)(h ? p.vb : p.kb) + ((size_t)g * SK + SK - WIN + (j & 7) * 512) * 256 : (h & 1) ? vb : kb;
      const int cnt_h = h < 2 ? NTB : isb ? NSTAT - NTB : NSTAT;
#pragma unroll 1
      for (int k = cnt_h; k > 0; --k) {
        if (h == 3 && k <= 4) {
          if (k == 4) lds_wait_ge(FL + F_QA * 4, 1u);
          ask(NSTAT + 4 - k);
        }
        unit(src + (h < 2 ? (cnt_h - k) * 8192 : place(j, cnt_h - k)), h & 1);
      }
    }
    unsigned o0 = answer(NSTAT), o1 = 0, p0 = 0, p1 = 0;
    int n = NSTAT;
#pragma unroll 1
    for (bool first = true; n < nt; n += 2, first = false) {
      if (n + 4 < nt) ask(n + 4);
      unit(kb + o0, false);
      const int nx = first ? n + 1 : n + 3;
      if (nx < nt) p1 = answer(nx);
      if (first) o1 = p1;
      if (n + 1 < nt) {
        if (n + 5 < nt) ask(n + 5);
        unit(kb + o1, false);
      }
      if (first && n + 2 < nt) p0 = answer(n + 2);
      unit(vb + o0, true);
      if (first && n + 3 < nt) p1 = answer(n + 3);
      if (n + 1 < nt) unit(vb + o1, true);
      o0 = p0; o1 = p1;
      if (n + 4 < nt) p0 = answer(n + 4);
    }
    n = nt - 1;
    const int U_WO0 = u, U_TOT = U_WO0 + U_WO;
    const int sl = lane >> 3, gi = lane & 7, jc = j & 31;
    v4u ls, ov[8];
    const unsigned sm = (P ? W_LSB + (g * 8 + sl) * 128 : W_X + (g * 64 + 8 * gi + sl) * XB + X_LS) + (jc >> 2) * 16;
    const unsigned so = P ? W_POB + (g * 32 + jc) * 1024 + lane * 16 : W_X + (g * 64 + sl) * XB + X_PO + jc * 128 + gi * 16;
    unsigned pend = P ? 0x30 : 0x3f;
    auto issue = [&]() {
      if (pend & 32u) GLD(ls, sm, 0);
      if (pend & 16u) GLD(ov[0], so, 0);
      if (pend & 1u) GLD(ov[1], so + 8 * XB, 0);
#pragma unroll
      for (int q = 1; q < 4; ++q)
        if (pend & (1u << q)) { GLD(ov[2 * q], so + 16 * q * XB, 0); GLD(ov[2 * q + 1], so + (16 * q + 8) * XB, 0); }
    };
    if (P) issue();
    {
      const GAS unsigned char* wb = (const GAS unsigned char*)(P ? p.wob : p.woa) + (size_t)(g * 1024 + (j & 31) * 32) * 2048;
#pragma unroll 1
      for (int w = 0; w < U_WO; ++w) {
        u = U_WO0 + w;
        pre(u); pace(u, INFL);
        const unsigned d = (u & (NSLOT - 1)) * SLOT;
#pragma unroll
        for (int hf = 0; hf < 2; ++hf) {
          const unsigned wr = (w + rot) & 7;
          const GAS unsigned char* s = wb + wr * 8192 + hf * 4096;
          const unsigned a0 = (lane ^ ((4 * (wr & 1) + 2 * hf) << 1)) << 4, a1 = a0 ^ 32;
          DMA4(s, a0, a0, a1, a1, d + hf * 4096);
        }
      }
    }
    v2u tg; asm volatile("ds_read_b64 %0, %1\n\ts_waitcnt lgkmcnt(0)" : "=v"(tg) : "v"(FL + F_TAG * 4) : "memory");
    const unsigned T0 = __builtin_amdgcn_readfirstlane(tg.x), T1 = __builtin_amdgcn_readfirstlane(tg.y);
    auto bad = [&](const v4u& v, unsigned a) { return __builtin_amdgcn_bitop3_b32(v.w, T1, __builtin_amdgcn_bitop3_b32(v.y, T0, a, 0xBE), 0xBE); };
    static_assert(OTRIG >= U_WO - 3 && OTRIG <= U_WO, "");
#pragma unroll 1
    for (int i = 0; i < SPIN_LDS && pub < U_TOT + (P ? 0 : 1); ++i) {
      if (!P && pub >= U_WO0 + OTRIG && lds_rd(FL + F_PUB * 4)) break;
      if (pub < U_TOT) { ladder(U_TOT - pub - 1); ++pub; lds_wr(FL + F_LANDED * 4, pub); }
    }
    if (!P) {
      issue();
      while (pub < U_TOT) {
        const int o = U_TOT - pub - 1;
        if (o == 2) VMCNT(25); else if (o == 1) VMCNT(17); else VMCNT(9);
        ++pub; lds_wr(FL + F_LANDED * 4, pub);
      }
    }
    {
      const unsigned live = pend;
#pragma unroll 1
      for (int it = 0; it < SPIN_GLB; ++it) {
        if (it) issue();
        asm volatile("s_waitcnt vmcnt(0)" : "+v"(ov[0]), "+v"(ov[1]), "+v"(ov[2]), "+v"(ov[3]), "+v"(ov[4]), "+v"(ov[5]), "+v"(ov[6]), "+v"(ov[7]), "+v"(ls) :: "memory");
        unsigned x = bad(ov[0], 0u);
        if (P == 0) x |= bad(ov[1], bad(ov[2], bad(ov[3], 0u))) | bad(ov[4], bad(ov[5], 0u)) | bad(ov[6], bad(ov[7], 0u));
        if (__builtin_expect(!__any(bad(ls, x >> 16) != 0u), 1)) break;
        pend = __any(bad(ls, 0u) != 0u) ? 32u : 0u;
        if (__any((bad(ov[0], 0u) >> 16) != 0u)) pend |= 16u;
        if (__any((bad(ov[1], 0u) >> 16) != 0u)) pend |= 1u;
#pragma unroll
        for (int q = 1; q < 4; ++q) if (__any((bad(ov[2 * q], bad(ov[2 * q + 1], 0u)) >> 16) != 0u)) pend |= 1u << q;
        pend &= live;
      }
      float M = f_of(ls.x);
      M = vmax(M, dpp<ROR1>(M)); M = vmax(M, dpp<ROR2>(M)); M = vmax(M, dpp<ROR4>(M)); M = vmax(M, dpp<ROR8>(M)); M = x16_max(M);
      const float w = ex2((f_of(ls.x) - M) * LOG2E) * f_of(ls.z);
      float W = w;
      v4f o;
      if (P == 0) {
        o = v4f{0, 0, 0, 0};
#define CMB(i) { int wi = __builtin_amdgcn_mov_dpp(__builtin_bit_cast(int, w), NEWBCAST + i, 0xf, 0xf, true); \
        wi = __builtin_amdgcn_update_dpp(wi, __builtin_bit_cast(int, w), NEWBCAST + 8 + i, 0xf, 0xc, false); o += unpack4(ov[i]) * __builtin_bit_cast(float, wi); }
        CMB(0) CMB(1) CMB(2) CMB(3) CMB(4) CMB(5) CMB(6) CMB(7)
#undef CMB
        W += dpp<ROR1>(W); W += dpp<ROR2>(W); W += dpp<ROR4>(W);
      } else o = unpack4(ov[0]) * w;
      W += dpp<ROR8>(W); W = x16_sum(W);
      const float iw = __builtin_amdgcn_rcpf(W);
      float r = x16_sum4(o[0], o[1], o[2], o[3]);
      r = (r + dpp<ROR8>(r)) * iw;
      if (!(lane & 8)) asm volatile("ds_write_b16 %0, %1" :: "v"(L_O + gi * 8 + (lane >> 4) * 2), "v"(pk_bf16(r, r)) : "memory");
      lds_wr(FL + F_O * 4, 1u);
    }
    {
      const unsigned sx = W_X + (P * 32 + (lane >> 1)) * XB + prod * 32 + (lane & 1) * 16;
#pragma unroll 1
      for (int i = 0; i < SPIN_LDS; ++i) {
        v4u w; asm volatile("ds_read_b128 %0, %1\n\ts_waitcnt lgkmcnt(0)" : "=v"(w) : "v"(FL + F_ACK * 4) : "memory");
        if (__builtin_amdgcn_readfirstlane(w.x & w.y & w.z & w.w)) break;
      }
      pend = 0xf;
#pragma unroll 1
      for (int it = 0; it < SPIN_GLB; ++it) {
#pragma unroll
        for (int q = 0; q < 4; ++q) if (pend & (1u << q)) GLD(ov[q], sx + q * 64 * XB, 0);
        asm volatile("s_waitcnt vmcnt(0)" : "+v"(ov[0]), "+v"(ov[1]), "+v"(ov[2]), "+v"(ov[3]) :: "memory");
        if (!__any(((bad(ov[0], bad(ov[1], 0u)) | bad(ov[2], bad(ov[3], 0u))) >> 16) != 0u)) break;
        pend = 0;
#pragma unroll
        for (int q = 0; q < 4; ++q) if (__any((bad(ov[q], 0u) >> 16) != 0u)) pend |= 1u << q;
      }
      const v4f s = (unpack4(ov[0]) + unpack4(ov[1])) + (unpack4(ov[2]) + unpack4(ov[3]));
      float r = x16_sum4(s[0], s[1], s[2], s[3]);
      r += dpp<ROR2>(r); r += dpp<ROR4>(r); r += dpp<ROR8>(r);
      if ((lane & 14) == 0) ((GAS u16*)p.out)[P * 1024 + (prod >> 5) * 256 + (lane >> 4) * 64 + (prod & 31) * 2 + (lane & 1)] = (u16)pk_bf16(r, r);
      if (b == 0 && lane == 0) __builtin_amdgcn_raw_buffer_store_b64(v2u{T0, T1}, ws, W_EPOCH, 0, 16);
    }
    return;
  }

  const int c = wave - 1;
  const int r16 = lane & 15, kg = lane >> 4;
  const int bq = (lane >> 3) & 1, hh = lane & 7;
  SCHED(); __builtin_amdgcn_s_barrier(); SCHED();
  v2u ep = __builtin_amdgcn_raw_buffer_load_b64(ws, W_EPOCH, 0, 16);
  v4u xv = {0, 0, 0, 0};
  if (c == 0) {
    xv = *(const GAS v4u*)((const GAS unsigned char*)(P ? p.xb : p.xa) + (j & 1) * 1024 + lane * 16);
    if (j < 4 && lane == 0) __builtin_amdgcn_raw_buffer_store_b32(0u, ws, W_CNT + (g * 4 + j) * 256, 0, 16);
  } else if (c == 1) {
    xv.x = __builtin_amdgcn_raw_buffer_load_b32(ws, W_X + (g * 64 + j) * XB + lane * 128, 0, 16);
  } else if (c == 2) {
    if (lane == 0) xv.x = __builtin_amdgcn_raw_buffer_load_b32(ws, W_X + (g * 64 + j) * XB + X_LS, 0, 16);
    if (lane < 4) xv.y = ((const GAS unsigned*)p.out)[P * 512 + (prod >> 5) * 128 + lane * 32 + (prod & 31)];
  } else if ((b >> 3) < CPF) xv = *(const GAS v4u*)((pc0 & ~255ull) + (b >> 3) * 1024 + lane * 16);
  asm volatile("s_waitcnt vmcnt(0)" : "+v"(ep), "+v"(xv) :: "memory");
  const unsigned T0 = __builtin_amdgcn_readfirstlane(ep.x) + 0x10001u, T1 = __builtin_amdgcn_readfirstlane(ep.y) + 0x9E3779B9u;
  if (c == 1) asm volatile("ds_write_b64 %0, %1" :: "v"(FL + F_TAG * 4), "v"(v2u{T0, T1}) : "memory");
  unsigned landed = 0, ntot = 0;
  auto poll = [&]() {
    v2u w; asm volatile("ds_read_b64 %0, %1\n\ts_waitcnt lgkmcnt(0)" : "=v"(w) : "v"(FL) : "memory");
    landed = __builtin_amdgcn_readfirstlane(w.x); ntot = __builtin_amdgcn_readfirstlane(w.y);
  };
  auto need_unit = [&](int u) {
#pragma unroll 1
    for (int i = 0; landed <= (unsigned)u && i < SPIN_LDS; ++i) { if (i) __builtin_amdgcn_s_sleep(1); poll(); }
  };
  auto release = [&](int u) { lds_wr(FL + (F_FREE + (u & (NSLOT - 1))) * 4, u + 1); };
  auto bad = [&](const v4u& v, unsigned a) { return __builtin_amdgcn_bitop3_b32(v.w, T1, __builtin_amdgcn_bitop3_b32(v.y, T0, a, 0xBE), 0xBE); };

  const unsigned koff = (16 * (kg >> 1) + r16) * 256 + (((kg & 1) ^ r16) << 4);
  const int vrow = 4 * kg + (r16 >> 2);
  const unsigned vsw = ((((r16 & 3) >> 1) ^ ((vrow & 7) << 1)) << 4) + 8 * (r16 & 1);
  const unsigned voff = vrow * 256 + vsw;
  auto read_k = [&](int u, v4u (&kf)[8]) {
    need_unit(u);
    const unsigned a = (u & (NSLOT - 1)) * SLOT + koff;
#pragma unroll
    for (int s = 0; s < 8; ++s) kf[s] = lds128(a ^ (s * 32));
    SCHED(); release(u); SCHED();
  };
  auto read_v = [&](int u, v4u (&vf)[8]) {
    need_unit(u);
    const unsigned a = (u & (NSLOT - 1)) * SLOT + voff;
#pragma unroll
    for (int s = 0; s < 8; ++s) { const v2u x = ldstr(a ^ (s * 32)), y = ldstr((a ^ (s * 32)) + 4096); vf[s] = v4u{x.x, x.y, y.x, y.y}; }
    SCHED(); release(u); SCHED();
  };

  if (c == 0) {
    *(LAS v4u*)(size_t)(L_X + lane * 16) = xv;
    v4f a[4];
#pragma unroll
    for (int i = 0; i < 4; ++i) a[i] = v4f{0, 0, 0, 0};
    const unsigned toff = (8 * kg + (r16 >> 2)) * 128 + ((((r16 & 3) >> 1) ^ ((r16 >> 2) & 2) ^ ((kg & 1) << 2)) << 4) + 8 * (r16 & 1);
#pragma unroll 1
    for (int u = 0; u < U_WQ; ++u) {
      need_unit(u);
      const unsigned base = u * SLOT + toff;
      v4u xf[2], w[2][4];
#pragma unroll
      for (int s = 0; s < 2; ++s) {
        xf[s] = lds128(L_X + ((u + rot) & 7) * 128 + s * 64 + kg * 16);
#pragma unroll
        for (int i = 0; i < 4; ++i) { const v2u p0 = ldstr((base ^ (i * 32)) + s * 4096), p1 = ldstr((base ^ (i * 32)) + s * 4096 + 512); w[s][i] = v4u{p0.x, p0.y, p1.x, p1.y}; }
      }
      SCHED(); release(u); SCHED();
#pragma unroll
      for (int s = 0; s < 2; ++s)
#pragma unroll
        for (int i = 0; i < 4; ++i) a[i] = mfma(xf[s], w[s][i], a[i]);
    }
    if (lane < 32)
      GST((v4u{u_of(kg ? a[2][0] : a[0][0]), T0, u_of(kg ? a[3][0] : a[1][0]), T1}), W_QG + ((P * 4 + g) * 2 + (j & 1)) * 8192 + (((j >> 1) & 15) * 32 + lane) * 16);
  }
  v4u k0[8], v0[8], k1[8], v1[8];
  read_k(U_TILE0 + c, k0); read_k(U_TILE0 + 4 + c, k1);
  const bool qg = c == 3 || (c == 2 && isb);
  {
    const int PP = 3 - c;
    const unsigned src = W_QG + (PP * 4 + g) * 2 * 8192 + lane * 16;
    v4u h0[8], h1[8];
    unsigned pend = 0xf;
    auto issue = [&]() {
#pragma unroll
      for (int q = 0; q < 4; ++q)
        if (pend & (1u << q)) {
          GLD(h0[2 * q], src + q * 2048, 0); GLD(h0[2 * q + 1], src + q * 2048, 1024);
          GLD(h1[2 * q], src + q * 2048 + 8192, 0); GLD(h1[2 * q + 1], src + q * 2048 + 8192, 1024);
        }
    };
    if (qg) { need_unit(QTRIG - 1); issue(); }
    read_k(U_TILE0 + 8 + c, v0); read_k(U_TILE0 + 12 + c, v1);
    if (qg) {
#pragma unroll 1
      for (int it = 0; it < SPIN_GLB; ++it) {
        if (it) issue();
        asm volatile("s_waitcnt vmcnt(0)" : "+v"(h0[0]), "+v"(h0[1]), "+v"(h0[2]), "+v"(h0[3]), "+v"(h0[4]), "+v"(h0[5]), "+v"(h0[6]), "+v"(h0[7]),
                     "+v"(h1[0]), "+v"(h1[1]), "+v"(h1[2]), "+v"(h1[3]), "+v"(h1[4]), "+v"(h1[5]), "+v"(h1[6]), "+v"(h1[7]) :: "memory");
        pend = 0;
#pragma unroll
        for (int q = 0; q < 4; ++q) if (__any(bad(h0[2 * q], bad(h0[2 * q + 1], bad(h1[2 * q], bad(h1[2 * q + 1], 0u)))) != 0u)) pend |= 1u << q;
        if (!pend) break;
      }
#pragma unroll
      for (int i = 0; i < 8; ++i) {
        const unsigned pk = pk_bf16(f_of(h0[i].x) + f_of(h1[i].x), f_of(h0[i].z) + f_of(h1[i].z));
        const unsigned a = L_Q + PP * QSZ + i * QROW + (32 * kg + r16) * 2;
        *(LAS u16*)(size_t)a = (u16)pk; *(LAS u16*)(size_t)(a + 32) = (u16)(pk >> 16);
      }
      lds_wr(FL + (F_QA + PP) * 4, 1u);
    }
  }

  v4u qf[8];
  v4f acc[8];
  float m, l, lt;
  unsigned phase = isb ? 1u : 2u;
  auto half = [&](int cc) { const v4f x = acc[cc], y = acc[4 + cc]; return v4f{bq ? y[0] : x[0], bq ? y[1] : x[1], bq ? y[2] : x[2], bq ? y[3] : x[3]}; };
  auto load_q = [&]() {
    lds_wait_ge(FL + (F_QA + 2 - phase) * 4, 1u);
    const unsigned a = L_Q + (2 - phase) * QSZ + hh * QROW + (kg & 1) * 16;
    const unsigned mk = ((kg >> 1) == bq) ? 0xffffffffu : 0u;
#pragma unroll
    for (int s = 0; s < 8; ++s) qf[s] = lds128(a + s * 32) & mk;
#pragma unroll
    for (int s = 0; s < 8; ++s) acc[s] = v4f{0, 0, 0, 0};
    m = -INFINITY; l = 0.f;
  };
  auto scores = [&](const v4u (&kf)[8]) {
    v4f s0 = {0, 0, 0, 0}, s1 = {0, 0, 0, 0};
#pragma unroll
    for (int s = 0; s < 4; ++s) { s0 = mfma(kf[2 * s], qf[2 * s], s0); s1 = mfma(kf[2 * s + 1], qf[2 * s + 1], s1); }
    const unsigned r01 = pk_bf16(s0[0] + s1[0], s0[1] + s1[1]), r23 = pk_bf16(s0[2] + s1[2], s0[3] + s1[3]);
    return v2u{pk_bf16(lo_f(r01) * SCALE, hi_f(r01) * SCALE), pk_bf16(lo_f(r23) * SCALE, hi_f(r23) * SCALE)};
  };
  auto max4 = [&](v2u t, float x) { return vmax(vmax(vmax(lo_f(t.x), hi_f(t.x)), x), vmax(lo_f(t.y), hi_f(t.y))); };
  auto head_max = [&](float x) { x = vmax(x, dpp<ROR8>(x)); return x16_max(x); };
  v4u pf;
  auto probs = [&](v2u t) {
    const float p0 = ex2((lo_f(t.x) - m) * LOG2E), p1 = ex2((hi_f(t.x) - m) * LOG2E), p2 = ex2((lo_f(t.y) - m) * LOG2E), p3 = ex2((hi_f(t.y) - m) * LOG2E);
    l += (p0 + p1) + (p2 + p3);
    const unsigned w01 = pk_bf16(p0, p1), w23 = pk_bf16(p2, p3);
    const unsigned x01 = u_of(dpp<ROR8>(f_of(w01))), x23 = u_of(dpp<ROR8>(f_of(w23)));
    pf = bq ? v4u{x01, x23, w01, w23} : v4u{w01, w23, x01, x23};
    lt = x16_sum(l); lt += dpp<ROR8>(lt);
    asm volatile("" : "+v"(pf), "+v"(lt));
  };
  auto pv = [&]() {
#pragma unroll
    for (int s = 0; s < 8; ++s) acc[s] = mfma(v0[s], pf, acc[s]);
  };
  const unsigned sc = L_S + c * (7 * 512) + lane * 8;
  load_q();
  {
    const v2u t0 = scores(k0), t1 = scores(k1), t2 = scores(v0), t3 = scores(v1);
    *(LAS v2u*)(size_t)sc = t0; *(LAS v2u*)(size_t)(sc + 512) = t1; *(LAS v2u*)(size_t)(sc + 1024) = t2; *(LAS v2u*)(size_t)(sc + 1536) = t3;
    m = max4(t3, max4(t2, max4(t1, max4(t0, m))));
  }
  int n, i0 = 4;
  unsigned lim;
  bool last;
  for (;;) {
    const int nb = phase == 1 ? NTB / 4 : isb ? (NSTAT - NTB) / 4 : NSTAT / 4;
    const int ub = U_TILE0 + c + (phase == 2 && isb ? 2 * NTB : 0);
#pragma unroll 1
    for (int i = i0; i < nb; ++i) {
      read_k(ub + 4 * i, k0);
      const v2u t = scores(k0);
      *(LAS v2u*)(size_t)(sc + i * 512) = t;
      m = max4(t, m);
    }
    m = head_max(m);
#pragma unroll 1
    for (int i = 0; i < nb; ++i) {
      probs(*(const LAS v2u*)(size_t)(sc + i * 512));
      read_v(ub + 4 * (nb + i), v0);
      pv();
    }
    n = (phase == 1 ? NTB : NSTAT) + c;
    lim = NTB;
    if (phase == 2) {
#pragma unroll 1
      for (;; n += 4) {
        const unsigned uk = U_TILE0 + 2 * n - (c & 1);
#pragma unroll 1
        for (int w = 0; landed <= uk && !(ntot != 0u && (unsigned)n >= ntot); ++w) {
          if (w == SPIN_LDS) { ntot = n; break; }
          if (w) __builtin_amdgcn_s_sleep(1);
          poll();
        }
        if (ntot != 0u && (unsigned)n >= ntot) break;
        read_k(uk, k0);
        const v2u t = scores(k0);
        const float mn = head_max(max4(t, m)), al = ex2((m - mn) * LOG2E);
        m = mn; l *= al;
#pragma unroll
        for (int s = 0; s < 8; ++s) acc[s] *= al;
        probs(t);
        asm volatile("" : "+v"(acc[0]), "+v"(acc[1]), "+v"(acc[2]), "+v"(acc[3]), "+v"(acc[4]), "+v"(acc[5]), "+v"(acc[6]), "+v"(acc[7]));
        read_v(uk + ((unsigned)n + 1 == ntot && !(c & 1) ? 1 : 2), v0);
        pv();
      }
      lim = ntot;
    }
    const int st = n - lim;
    last = st == 3;
    if (last) {
      v4f sv[3][4]; v2f ml[3];
#pragma unroll 1
      for (int i = 0; i < SPIN_LDS; ++i) {
        v4u w; asm volatile("ds_read_b128 %0, %1\n\ts_waitcnt lgkmcnt(0)" : "=v"(w) : "v"(FL + F_ST * 4) : "memory");
        if (__builtin_amdgcn_readfirstlane(w.x & w.y & w.z) == phase) break;
      }
#pragma unroll
      for (int s = 0; s < 3; ++s) {
        ml[s] = *(const LAS v2f*)(size_t)(L_ST + s * STSZ + 4096 + hh * 8);
#pragma unroll
        for (int cc = 0; cc < 4; ++cc) sv[s][cc] = __builtin_bit_cast(v4f, lds128(L_ST + s * STSZ + cc * 1024 + lane * 16));
      }
      SCHED(); lds_wr(FL + F_BM * 4, 1u);
      const float mj = vmax(vmax(m, ml[0].x), vmax(ml[1].x, ml[2].x));
      float f = ex2((m - mj) * LOG2E), f0 = ex2((ml[0].x - mj) * LOG2E), f1 = ex2((ml[1].x - mj) * LOG2E), f2 = ex2((ml[2].x - mj) * LOG2E);
      lt = fmaf(lt, f, ml[0].y * f0) + fmaf(ml[1].y, f1, ml[2].y * f2);
      const float il = __builtin_amdgcn_rcpf(lt);
      f *= il; f0 *= il; f1 *= il; f2 *= il;
      const unsigned cs = phase == 1 ? 8 * 128 : 128;
      const unsigned a = (phase == 1 ? W_POB + g * 32 * 1024 + (j & 7) * 128 : W_X + (g * 64 + j) * XB + X_PO) + (hh * 4 + 2 * bq) * cs + kg * 16;
#pragma unroll
      for (int cc = 0; cc < 4; ++cc) GST(pack4((half(cc) * f + sv[0][cc] * f0) + (sv[1][cc] * f1 + sv[2][cc] * f2), T0, T1), a + (cc >> 1) * cs + (cc & 1) * 64);
      if (lane < 8) GST((v4u{u_of(mj), T0, u_of(lt), T1}), (phase == 1 ? W_LSB + (g * 8 + (j & 7)) * 128 : W_X + (g * 64 + j) * XB + X_LS) + lane * 16);
    } else {
      if (isb && phase == 2) lds_wait_ge(FL + F_BM * 4, 1u);
      const unsigned sa = L_ST + st * STSZ;
      if (lane < 8) *(LAS v2f*)(size_t)(sa + 4096 + lane * 8) = v2f{m, lt};
#pragma unroll
      for (int cc = 0; cc < 4; ++cc) *(LAS v4f*)(size_t)(sa + cc * 1024 + lane * 16) = half(cc);
      lds_wr(FL + (F_ST + st) * 4, phase);
    }
    if (phase == 2) break;
    phase = 2; i0 = 0; load_q();
  }
  if (last) { VMCNT(0); lds_wr(FL + F_PUB * 4, 1u); }
  const int U_WO0 = U_TILE0 + 2 * lim, U_TOT = U_WO0 + U_WO;

  v4u wf[16];
  need_unit(U_TOT - 1);
  {
    const unsigned a0 = ((U_WO0 + ((kg - rot) & 7)) & (NSLOT - 1)) * SLOT + (r16 >> 2) * 2048 + vsw + c * 512;
    const unsigned a1 = ((U_WO0 + ((4 + kg - rot) & 7)) & (NSLOT - 1)) * SLOT + (r16 >> 2) * 2048 + vsw + c * 512;
#pragma unroll
    for (int blk = 0; blk < 16; ++blk) {
      const v2u x = ldstr((a0 ^ ((blk & 7) * 32)) + (blk >> 3) * 256), y = ldstr((a1 ^ ((blk & 7) * 32)) + (blk >> 3) * 256);
      wf[blk] = v4u{x.x, x.y, y.x, y.y};
    }
    SCHED();
  }
  {
    v2u oa, ob;
#pragma unroll 1
    for (int i = 0; i < SPIN_LDS; ++i) {
      unsigned f;
      asm volatile("ds_read_b32 %0, %3\n\tds_read_b64 %1, %4\n\tds_read_b64 %2, %4 offset:32\n\ts_waitcnt lgkmcnt(0)"
                   : "=&v"(f), "=&v"(oa), "=&v"(ob) : "v"(FL + F_O * 4), "v"(L_O + kg * 8) : "memory");
      if (__builtin_amdgcn_readfirstlane(f)) break;
    }
    v4f oc[4];
#pragma unroll
    for (int i = 0; i < 4; ++i) oc[i] = v4f{0, 0, 0, 0};
#pragma unroll
    for (int q = 0; q < 4; ++q) {
      const unsigned mk = (r16 == 4 * q) ? 0xffffffffu : 0u;
      const v4u of = {oa.x & mk, oa.y & mk, ob.x & mk, ob.y & mk};
#pragma unroll
      for (int i = 0; i < 4; ++i) oc[i] = mfma(of, wf[4 * i + q], oc[i]);
    }
    GST(pack4(v4f{oc[0][0], oc[1][0], oc[2][0], oc[3][0]}, T0, T1), W_X + (g * 64 + j) * XB + c * 1024 + lane * 16);
    VMCNT(0);
    lds_wr(FL + (F_ACK + c) * 4, 1u);
  }
}

extern "C" void kernel_launch(void* const* d_in, const int* in_sizes, int n_in, void* d_out, int out_size, void* d_ws, size_t ws_size, hipStream_t stream) {
  Params p;
  p.xa = (const u16*)d_in[0]; p.ka = (const u16*)d_in[1]; p.va = (const u16*)d_in[2]; p.wqa = (const u16*)d_in[3]; p.woa = (const u16*)d_in[4];
  p.xb = (const u16*)d_in[5]; p.kb = (const u16*)d_in[6]; p.vb = (const u16*)d_in[7]; p.wqb = (const u16*)d_in[8]; p.wob = (const u16*)d_in[9];
  p.out = (u16*)d_out; p.ws = (unsigned char*)d_ws + ws_base(ws_size);
  (void)hipFuncSetAttribute((const void*)fused_decode, hipFuncAttributeMaxDynamicSharedMemorySize, LDS_BYTES);
  fused_decode<<<NWG, NTHR, LDS_BYTES, stream>>>(p);
}
```
